```python
import torch

def _softmax(x, axis=-1):
    return torch.softmax(x, dim=axis)
def _gelu(x):
    return torch.nn.functional.gelu(x, approximate="tanh")
def _silu(x):
    return torch.nn.functional.silu(x)
def _sigmoid(x):
    return torch.sigmoid(x)
def _layernorm(x, axis=-1, eps=1e-5):
    m = x.mean(dim=axis, keepdim=True)
    v = x.var(dim=axis, keepdim=True, unbiased=False)
    return (x - m) / torch.sqrt(v + eps)
def _scatter_add(tgt, idx, src, axis=0):
    return tgt.clone().index_add_(axis, idx, src)
def _segment_sum(src, seg, n):
    out = torch.zeros((n,) + src.shape[1:], dtype=src.dtype)
    return out.index_add_(0, seg, src)
def _pad(x, pw):
    flat = tuple(v for pair in reversed(pw) for v in pair)
    return torch.nn.functional.pad(x, flat)

kernel_name = 'parallel_sum__sliding_block_cached__mixer_channel_mlp__decode'

def setup_inputs():

    _w = (lambda a: (lambda t: t.to(torch.bfloat16) if t.is_floating_point() else t)(torch.as_tensor(a)))
    def _A_setup():
        B, Sq, D = 1, 1, 576
        Hq, Hkv, Sk, Dh = 32, 16, 65536, 128
        assert Hq % Hkv == 0, (Hq, Hkv)
        return {
            "x": _w((lambda *s: torch.randn(*s, dtype=torch.bfloat16) * 0.05)(B, Sq, D)),
            "k_cache": _w((lambda *s: torch.randn(*s, dtype=torch.bfloat16) * 3.2)(B, Hkv, Sk, Dh)),
            "v_cache": _w((lambda *s: torch.randn(*s, dtype=torch.bfloat16) * 0.4)(B, Hkv, Sk, Dh)),
            "wq": _w((lambda *s: torch.randn(*s, dtype=torch.bfloat16) * 3.2)(D, Hq * Dh) * (D ** -0.5)),
            "wo": _w((lambda *s: torch.randn(*s, dtype=torch.bfloat16) * 0.4)(Hq * Dh, D) * ((Hq * Dh) ** -0.5)),
        }
    _A_in = _A_setup()

    def _B_setup():
        B, S, D, F = 1, 1, 576, 4608
        return {
            "x": _w((lambda *s: torch.randn(*s, dtype=torch.bfloat16) * 0.05)(B, S, D)),
            "w1": _w((lambda *s: torch.randn(*s, dtype=torch.bfloat16) * 3.2)(D, F) * (D ** -0.5)),
            "b1": _w((lambda *s: torch.randn(*s, dtype=torch.bfloat16) * 0.8)(F)),
            "w2": _w((lambda *s: torch.randn(*s, dtype=torch.bfloat16) * 0.1)(F, D) * (F ** -0.5)),
            "b2": _w((lambda *s: torch.randn(*s, dtype=torch.bfloat16) * 0.1)(D)),
        }
    _B_in = _B_setup()

    return {
        "A_x": _A_in["x"],
        "A_k_cache": _A_in["k_cache"],
        "A_v_cache": _A_in["v_cache"],
        "A_wq": _A_in["wq"],
        "A_wo": _A_in["wo"],
        "B_x": _B_in["x"],
        "B_w1": _B_in["w1"],
        "B_b1": _B_in["b1"],
        "B_w2": _B_in["w2"],
        "B_b2": _B_in["b2"],
    }

def reference(A_x, A_k_cache, A_v_cache, A_wq, A_wo, B_x, B_w1, B_b1, B_w2, B_b2):
    def _A_ref(x, k_cache, v_cache, wq, wo):
        B, Sq, D = x.shape
        Hq, Dh = 32, 128
        Hkv, Sk = k_cache.shape[1], k_cache.shape[2]
        W = min(4096, max(256, Sk // 4))
        rep = Hq // Hkv
        q = (lambda x, *d: x.permute(*d))((x @ wq).reshape(B, Sq, Hq, Dh), 0, 2, 1, 3)
        k = (lambda x, n, axis: torch.repeat_interleave(x, n, dim=axis))(k_cache[:, :, Sk - W:, :], rep, axis=1)
        v = (lambda x, n, axis: torch.repeat_interleave(x, n, dim=axis))(v_cache[:, :, Sk - W:, :], rep, axis=1)
        scores = torch.einsum("bhqd,bhkd->bhqk", q, k) * (Dh ** -0.5)
        p = _softmax(scores, axis=-1)
        o = torch.einsum("bhqk,bhkd->bhqd", p, v)
        return (lambda x, *d: x.permute(*d))(o, 0, 2, 1, 3).reshape(B, Sq, Hq * Dh) @ wo

    def _B_ref(x, w1, b1, w2, b2):
        y = _layernorm(x, axis=-1)
        h = _gelu(torch.einsum("bsd,df->bsf", y, w1) + b1)
        return torch.einsum("bsf,fd->bsd", h, w2) + b2

    x = A_x
    xn = _layernorm(x, axis=-1)
    return x + _A_ref(xn, A_k_cache, A_v_cache, A_wq, A_wo) + _B_ref(xn, B_w1, B_b1, B_w2, B_b2)
```

```cpp
#include <hip/hip_runtime.h>
#include <hip/hip_bf16.h>
#include <stdint.h>

typedef unsigned short u16;
typedef unsigned u32x4 __attribute__((ext_vector_type(4)));
#define MIMIC 1

__device__ __forceinline__ float bf2f(u16 v) { return __uint_as_float(((unsigned)v) << 16); }
__device__ __forceinline__ float rbf(float f) {
    unsigned u = __float_as_uint(f); u = (u + 0x7fffu + ((u >> 16) & 1u)) & 0xffff0000u; return __uint_as_float(u);
}
__device__ __forceinline__ float R(float f) { return MIMIC ? rbf(f) : f; }

constexpr int D = 576, HQ = 32, HKV = 16, SK = 65536, DH = 128, W = 4096, F = 4608;

__device__ float block_sum(float v, float* red) {
    for (int o = 32; o > 0; o >>= 1) v += __shfl_xor(v, o);
    __syncthreads();
    if ((threadIdx.x & 63) == 0) red[threadIdx.x >> 6] = v;
    __syncthreads();
    float s = 0; for (int i = 0; i < (int)(blockDim.x >> 6); ++i) s += red[i];
    return s;
}
__device__ void ln(const float* src, float* dst, float* red) {
    float s = 0; for (int i = threadIdx.x; i < D; i += blockDim.x) s += src[i];
    float mean_f = block_sum(s, red) / D;
    float m = R(mean_f);
    float q = 0; for (int i = threadIdx.x; i < D; i += blockDim.x) { float d = src[i] - mean_f; q += d * d; }
    float v = R(block_sum(q, red) / D);
    float den = R(sqrtf(R(v + 1e-5f)));
    for (int i = threadIdx.x; i < D; i += blockDim.x) dst[i] = R(R(src[i] - m) / den);
    __syncthreads();
}

__global__ void __launch_bounds__(256) k1(const u16* x, const u16* wq, const u16* w1, const u16* b1, float* q, float* h) {
    __shared__ float xs[D], xn[D], yn[D], red[8], part[8][64];
    for (int i = threadIdx.x; i < D; i += 256) xs[i] = bf2f(x[i]);
    __syncthreads();
    ln(xs, xn, red);
    bool isq = blockIdx.x < 64;
    if (!isq) ln(xn, yn, red);
    const float* v = isq ? xn : yn;
    const u16* wmat = isq ? wq : w1; int ld = isq ? 4096 : F; int cb = isq ? blockIdx.x : blockIdx.x - 64;
    int c = threadIdx.x & 31, g = threadIdx.x >> 5;
    float a0 = 0, a1 = 0;
    for (int r = g * 72; r < g * 72 + 72; ++r) {
        unsigned w = *(const unsigned*)(wmat + (size_t)r * ld + cb * 64 + 2 * c);
        a0 += v[r] * __uint_as_float(w << 16); a1 += v[r] * __uint_as_float(w & 0xffff0000u);
    }
    part[g][2 * c] = a0; part[g][2 * c + 1] = a1;
    __syncthreads();
    if (threadIdx.x < 64) {
        float s = 0; for (int i = 0; i < 8; ++i) s += part[i][threadIdx.x];
        int col = cb * 64 + threadIdx.x;
        if (isq) q[col] = R(s);
        else {
            float t = R(R(s) + bf2f(b1[col]));
            float gl = 0.5f * t * (1.f + tanhf(0.7978845608028654f * (t + 0.044715f * t * t * t)));
            h[col] = R(gl);
        }
    }
}
__global__ void __launch_bounds__(256) k2(const u16* kc, const u16* vc, const float* q, float* pm, float* pl, float* pacc) {
    __shared__ float qs[2][DH], p[2][256], red[8];
    int g = blockIdx.x >> 4, s = blockIdx.x & 15;
    qs[threadIdx.x >> 7][threadIdx.x & 127] = q[g * 256 + threadIdx.x];
    __syncthreads();
    size_t key0 = (size_t)g * SK + (SK - W) + s * 256;
    const u16* kp = kc + (key0 + threadIdx.x) * DH;
    float s0 = 0, s1 = 0;
    for (int d = 0; d < DH; d += 8) {
        u32x4 w = *(const u32x4*)(kp + d);
        for (int j = 0; j < 4; ++j) { float a = __uint_as_float(w[j] << 16), b = __uint_as_float(w[j] & 0xffff0000u);
            s0 += a * qs[0][d + 2 * j] + b * qs[0][d + 2 * j + 1]; s1 += a * qs[1][d + 2 * j] + b * qs[1][d + 2 * j + 1]; }
    }
    s0 = R(R(s0) * 0.08838834764831845f); s1 = R(R(s1) * 0.08838834764831845f);
    float m0 = s0, m1 = s1;
    for (int o = 32; o > 0; o >>= 1) { m0 = fmaxf(m0, __shfl_xor(m0, o)); m1 = fmaxf(m1, __shfl_xor(m1, o)); }
    if ((threadIdx.x & 63) == 0) { red[threadIdx.x >> 6] = m0; red[4 + (threadIdx.x >> 6)] = m1; }
    __syncthreads();
    m0 = fmaxf(fmaxf(red[0], red[1]), fmaxf(red[2], red[3])); m1 = fmaxf(fmaxf(red[4], red[5]), fmaxf(red[6], red[7]));
    float p0 = __expf(s0 - m0), p1 = __expf(s1 - m1);
    p[0][threadIdx.x] = p0; p[1][threadIdx.x] = p1;
    float l0 = block_sum(p0, red); float l1 = block_sum(p1, red);
    int hh = threadIdx.x >> 7, d = threadIdx.x & 127;
    const u16* vp = vc + key0 * DH + d;
    float acc = 0;
    for (int k = 0; k < 256; ++k) acc += p[hh][k] * bf2f(vp[(size_t)k * DH]);
    int head = g * 2 + hh;
    pacc[((size_t)head * 16 + s) * DH + d] = acc;
    if (d == 0) { pm[head * 16 + s] = hh ? m1 : m0; pl[head * 16 + s] = hh ? l1 : l0; }
}
__global__ void __launch_bounds__(288) k3(const float* pm, const float* pl, const float* pacc, const float* h, const u16* wo, const u16* w2, float* part) {
    __shared__ float v[34];
    int r0 = blockIdx.x * 34;
    if (threadIdx.x < 34) {
        int r = r0 + threadIdx.x;
        if (r < 4096) {
            int head = r >> 7, d = r & 127; float M = -INFINITY;
            for (int s = 0; s < 16; ++s) M = fmaxf(M, pm[head * 16 + s]);
            float L = 0, o = 0;
            for (int s = 0; s < 16; ++s) { float w = __expf(pm[head * 16 + s] - M); L += w * pl[head * 16 + s]; o += w * pacc[((size_t)head * 16 + s) * DH + d]; }
            v[threadIdx.x] = R(o / L);
        } else v[threadIdx.x] = h[r - 4096];
    }
    __syncthreads();
    float aa0 = 0, aa1 = 0, m0 = 0, m1 = 0;
    for (int i = 0; i < 34; ++i) {
        int r = r0 + i;
        const u16* row = r < 4096 ? wo + (size_t)r * D : w2 + (size_t)(r - 4096) * D;
        unsigned w = *(const unsigned*)(row + 2 * threadIdx.x);
        float a = v[i] * __uint_as_float(w << 16), b = v[i] * __uint_as_float(w & 0xffff0000u);
        if (r < 4096) { aa0 += a; aa1 += b; } else { m0 += a; m1 += b; }
    }
    part[(size_t)blockIdx.x * 2 * D + 2 * threadIdx.x] = aa0; part[(size_t)blockIdx.x * 2 * D + 2 * threadIdx.x + 1] = aa1;
    part[(size_t)blockIdx.x * 2 * D + D + 2 * threadIdx.x] = m0; part[(size_t)blockIdx.x * 2 * D + D + 2 * threadIdx.x + 1] = m1;
}
__global__ void __launch_bounds__(64) k4(const float* part, const u16* x, const u16* b2, __hip_bfloat16* out) {
    int c = blockIdx.x * 64 + threadIdx.x;
    float a = 0, m = 0;
    for (int b = 0; b < 256; ++b) { a += part[(size_t)b * 2 * D + c]; m += part[(size_t)b * 2 * D + D + c]; }
    float r = R(R(bf2f(x[c]) + R(a)) + R(R(m) + bf2f(b2[c])));
    out[c] = __float2bfloat16(r);
}

extern "C" void kernel_launch(void* const* d_in, const int* in_sizes, int n_in, void* d_out, int out_size, void* d_ws, size_t ws_size, hipStream_t stream) {
    const u16* x = (const u16*)d_in[0]; const u16* kc = (const u16*)d_in[1]; const u16* vc = (const u16*)d_in[2];
    const u16* wq = (const u16*)d_in[3]; const u16* wo = (const u16*)d_in[4];
    const u16* w1 = (const u16*)d_in[6]; const u16* b1 = (const u16*)d_in[7]; const u16* w2 = (const u16*)d_in[8]; const u16* b2 = (const u16*)d_in[9];
    float* ws = (float*)d_ws;
    float* q = ws; float* h = ws + 4096; float* pm = ws + 16384; float* pl = pm + 512; float* pacc = ws + 32768; float* part = ws + 131072;
    k1<<<64 + 72, 256, 0, stream>>>(x, wq, w1, b1, q, h);
    k2<<<256, 256, 0, stream>>>(kc, vc, q, pm, pl, pacc);
    k3<<<256, 288, 0, stream>>>(pm, pl, pacc, h, wo, w2, part);
    k4<<<9, 64, 0, stream>>>(part, x, b2, (__hip_bfloat16*)d_out);
}
```

```cpp
#include <hip/hip_runtime.h>
#include <hip/hip_bf16.h>
#include <stdint.h>

typedef unsigned short u16;
typedef unsigned u32x4 __attribute__((ext_vector_type(4)));
typedef unsigned u32x2 __attribute__((ext_vector_type(2)));
typedef float f32x4 __attribute__((ext_vector_type(4)));
typedef float f32x2 __attribute__((ext_vector_type(2)));
typedef short bf16x8 __attribute__((ext_vector_type(8)));
typedef __bf16 bf16x2 __attribute__((ext_vector_type(2)));
typedef __amdgpu_buffer_rsrc_t rsrc_t;
typedef __attribute__((address_space(3))) unsigned lu32;
typedef __attribute__((address_space(3))) unsigned char lu8;

#define WL 16
#define BARN 50
#define RATE 30
#define LEAD 20
#define D1 110
#define QPROG 60
#define HPROG 140
#define HT 3
#define P2A 24
#define P2B 26
#define M2 8
#define CODEKB 24

constexpr int D = 576, SK = 65536, DH = 128, W = 4096, F = 4608;
constexpr int NW = 9, NA = 8, NWAVES = 3 + NW, NTHR = NWAVES * 64;
constexpr unsigned H2P = 2176;
constexpr unsigned WS_EPOCH = 0, WS_H1 = 4096, WS_H2 = WS_H1 + 16 * 8192, WS_H3 = WS_H2 + 256 * H2P, WS_HH = WS_H3 + 144 * 8192, WS_F2 = WS_HH + 144 * 256, WS_FM = WS_F2 + 16 * 128, WS_END = WS_FM + 16 * 512 + 8 * 2048;
constexpr int L_W = 0, L_E = 18 * 1024, L_K = 61 * 1024, L_V1 = L_K + 76 * 1024, L_MISC = L_V1 + 16 * 1024, L_E2 = L_K + 56 * 1024;
__host__ __device__ constexpr int vtile(int ks) { return ks < 2 ? L_V1 + ks * 8192 : L_W + (ks - 2) * 8192; }
constexpr int L_HP = L_W + 56 * 1024;
constexpr int L_XP = L_MISC, L_YP = L_XP + 1152, L_Q = L_YP + 1152, L_P = L_Q + 512, L_AM = L_P + 2 * 640, L_AL = L_AM + 128, L_FT = L_AL + 128,
              L_VEC = L_FT + 128, L_RED = L_VEC + 128, L_FLAG = L_RED + 8 * 64, L_XS = L_FLAG + 128, L_EP = L_XS + 1280, L_END = L_EP + 256;
constexpr int LDS_BYTES = L_END;
static_assert(LDS_BYTES <= 160 * 1024 && (L_P % 16) == 0 && (L_FLAG % 16) == 0 && (L_RED % 16) == 0 && (L_HP % 16) == 0, "lds");
constexpr int FL_Q = 0, FL_F = 2, FL_H = 3, FL_V = 4, FL_R = 5, FL_TAG = 6, FL_L = 7, FL_Y = 8, FL_PROG = 10  , FL_FQ = 15, FL_E2 = 16, FL_T2 = 17, FL_T2V = 18, FL_RI = 21, FL_G = 22, FL_FE = 23, FL_SN = 24;
constexpr unsigned SPIN_CAP = 1u << 19;
constexpr float LOG2E = 1.4426950408889634f;
#define STR2(x) #x
#define STR(x) STR2(x)
#define GLDS(src, dst) __builtin_amdgcn_global_load_lds((const unsigned*)(src), (lu32*)(dst), 16, 0, 2)
#define VMWAIT(n) asm volatile("s_waitcnt vmcnt(" STR(n) ")" ::: "memory")

__device__ __forceinline__ float bflo(unsigned w) { return __uint_as_float(w << 16); }
__device__ __forceinline__ float bfhi(unsigned w) { return __uint_as_float(w & 0xffff0000u); }
__device__ __forceinline__ float rbf(float f) { unsigned u = __float_as_uint(f); u = (u + 0x7fffu + ((u >> 16) & 1u)) & 0xffff0000u; return __uint_as_float(u); }
__device__ __forceinline__ unsigned pkbf(float lo, float hi) { return __builtin_bit_cast(unsigned, __builtin_convertvector(f32x2{lo, hi}, bf16x2)); }
template <int CTRL> __device__ __forceinline__ float dpp(float x) { return __builtin_bit_cast(float, __builtin_amdgcn_mov_dpp(__builtin_bit_cast(int, x), CTRL, 0xf, 0xf, true)); }
constexpr int XOR1 = 0xB1, XOR2 = 0x4E, HMIR = 0x141, ROR8 = 0x128, ROR4 = 0x124;
__device__ __forceinline__ float row_sum(float v) { v += dpp<XOR1>(v); v += dpp<XOR2>(v); v += dpp<HMIR>(v); v += dpp<ROR8>(v); return v; }
__device__ __forceinline__ float x16_sum(float x) {
    auto s = __builtin_amdgcn_permlane16_swap(__float_as_uint(x), __float_as_uint(x), false, false); x = __uint_as_float(s[0]) + __uint_as_float(s[1]);
    auto t = __builtin_amdgcn_permlane32_swap(__float_as_uint(x), __float_as_uint(x), false, false); return __uint_as_float(t[0]) + __uint_as_float(t[1]);
}
__device__ __forceinline__ float x16_max(float x) {
    auto s = __builtin_amdgcn_permlane16_swap(__float_as_uint(x), __float_as_uint(x), false, false); x = fmaxf(__uint_as_float(s[0]), __uint_as_float(s[1]));
    auto t = __builtin_amdgcn_permlane32_swap(__float_as_uint(x), __float_as_uint(x), false, false); return fmaxf(__uint_as_float(t[0]), __uint_as_float(t[1]));
}
__device__ __forceinline__ float wave_sum(float v) { return x16_sum(row_sum(v)); }
__device__ __forceinline__ float rs32(float a, float b) { auto t = __builtin_amdgcn_permlane32_swap(__float_as_uint(a), __float_as_uint(b), false, false); return __uint_as_float(t[0]) + __uint_as_float(t[1]); }
__device__ __forceinline__ float rs16(float a, float b) { auto t = __builtin_amdgcn_permlane16_swap(__float_as_uint(a), __float_as_uint(b), false, false); return __uint_as_float(t[0]) + __uint_as_float(t[1]); }
__device__ __forceinline__ float dot2(unsigned a, unsigned b, float c) { return __builtin_amdgcn_fdot2_f32_bf16(__builtin_bit_cast(bf16x2, a), __builtin_bit_cast(bf16x2, b), c, false); }
__device__ __forceinline__ void pair_perm(u32x4& A, u32x4& B) {
#pragma unroll
    for (int k = 0; k < 4; ++k) { const unsigned lo = __builtin_amdgcn_perm(B[k], A[k], 0x05040100u), hi = __builtin_amdgcn_perm(B[k], A[k], 0x07060302u); A[k] = lo; B[k] = hi; }
}
__device__ __forceinline__ void pair_fma(float (&acc)[8], const u32x4 A, const u32x4 B, unsigned v) {
#pragma unroll
    for (int k = 0; k < 4; ++k) { acc[2 * k] = dot2(A[k], v, acc[2 * k]); acc[2 * k + 1] = dot2(B[k], v, acc[2 * k + 1]); }
}
#define PIN4(x) asm volatile("" : "+v"(x))

__device__ __forceinline__ rsrc_t mk(const void* p, unsigned bytes) { return __builtin_amdgcn_make_buffer_rsrc((void*)p, 0, bytes, 0x00020000); }
#define CBAR() asm volatile("" ::: "memory")
#define LDSWAIT() asm volatile("s_waitcnt lgkmcnt(0)" ::: "memory")
#define SB() __builtin_amdgcn_sched_barrier(0)

struct Tag { unsigned t32, t8; };
__device__ __forceinline__ Tag mktag(unsigned t) { return Tag{t, (t * 0x9E3779B1u) >> 24}; }
__device__ __forceinline__ unsigned enc(float v, Tag t) { return ((__float_as_uint(v) + 0x80u) & 0xffffff00u) | t.t8; }
__device__ __forceinline__ float dec(unsigned w) { return __uint_as_float(w & 0xffffff00u); }
__device__ __forceinline__ bool good(unsigned lo, unsigned hi, Tag t) { return (hi == t.t32) & ((lo & 0xffu) == t.t8); }
__device__ __forceinline__ void st2(rsrc_t r, unsigned off, float a, float b, Tag t) { __builtin_amdgcn_raw_buffer_store_b128(u32x4{enc(a, t), t.t32, enc(b, t), t.t32}, r, off, 0, 16); }
__device__ __forceinline__ void st1(rsrc_t r, unsigned off, float a, Tag t) { __builtin_amdgcn_raw_buffer_store_b64(u32x2{enc(a, t), t.t32}, r, off, 0, 16); }
__device__ __forceinline__ void atl(u32x2& r, const unsigned char* p) { asm volatile("global_atomic_or_x2 %0, %1, %0, off sc0 sc1" : "+v"(r) : "v"(p) : "memory"); }
template <int N> __device__ __forceinline__ void apoll(const unsigned char* ws, const unsigned (&off)[N], float (&val)[N], bool (&ok)[N], Tag t) {
    for (unsigned spin = 0; spin < SPIN_CAP; ++spin) {
        bool all = true;
#pragma unroll
        for (int i = 0; i < N; ++i) all &= ok[i];
        if (__all(all)) break;
        u32x2 v[N];
#pragma unroll
        for (int i = 0; i < N; ++i) { v[i] = u32x2{0u, 0u}; if (!ok[i]) atl(v[i], ws + off[i]); }
        asm volatile("s_waitcnt vmcnt(0)" ::: "memory");
#pragma unroll
        for (int i = 0; i < N; ++i) { asm volatile("" : "+v"(v[i])); const bool n = !ok[i] && good(v[i][0], v[i][1], t); if (n) val[i] = dec(v[i][0]); ok[i] |= n; }
    }
}
template <int N> __device__ __forceinline__ void gather2p(rsrc_t rws, const unsigned (&off)[N], float (&a)[N], float (&b)[N], Tag t, lu32* issued, int lane) {
    bool ok[N];
#pragma unroll
    for (int i = 0; i < N; ++i) ok[i] = false;
    for (unsigned spin = 0; spin < SPIN_CAP; ++spin) {
        u32x4 v[N];
#pragma unroll
        for (int i = 0; i < N; ++i) v[i] = __builtin_amdgcn_raw_buffer_load_b128(rws, ok[i] ? 0x7fffff00u : off[i], 0, 16);
        if (spin == 0) { SB(); if (lane == 0) __hip_atomic_fetch_add(issued, 1u, __ATOMIC_RELAXED, __HIP_MEMORY_SCOPE_WORKGROUP); SB(); }
        bool all = true;
#pragma unroll
        for (int i = 0; i < N; ++i) { const bool n = !ok[i] && good(v[i][0], v[i][1], t) && good(v[i][2], v[i][3], t); if (n) { a[i] = dec(v[i][0]); b[i] = dec(v[i][2]); } ok[i] |= n; all &= ok[i]; }
        CBAR();
        if (__all(all)) break;
    }
}
template <int N> __device__ __forceinline__ void gather2(rsrc_t rws, const unsigned char* ws, const unsigned (&off)[N], float (&a)[N], float (&b)[N], Tag t, lu32* issued = nullptr, int lane = 0) {
    u32x4 v[N]; bool ok[2 * N]; float val[2 * N]; unsigned o2[2 * N];
#pragma unroll
    for (int i = 0; i < N; ++i) v[i] = __builtin_amdgcn_raw_buffer_load_b128(rws, off[i], 0, 16);
    if (issued) { SB(); if (lane == 0) __hip_atomic_fetch_add(issued, 1u, __ATOMIC_RELAXED, __HIP_MEMORY_SCOPE_WORKGROUP); SB(); }
#pragma unroll
    for (int i = 0; i < N; ++i) { ok[2 * i] = good(v[i][0], v[i][1], t); ok[2 * i + 1] = good(v[i][2], v[i][3], t); val[2 * i] = dec(v[i][0]); val[2 * i + 1] = dec(v[i][2]); o2[2 * i] = off[i]; o2[2 * i + 1] = off[i] + 8; }
    CBAR();
    apoll<2 * N>(ws, o2, val, ok, t);
#pragma unroll
    for (int i = 0; i < N; ++i) { a[i] = val[2 * i]; b[i] = val[2 * i + 1]; }
}

__device__ __forceinline__ unsigned now32() { return (unsigned)__builtin_amdgcn_s_memrealtime(); }
__device__ __forceinline__ void wait_until(unsigned t) { while ((int)(now32() - t) < 0) {} }
__device__ __forceinline__ void lds_spin_ge(lu32* f, unsigned n) { while (*(volatile lu32*)f < n) {} CBAR(); }
__device__ __forceinline__ void lds_wait_ge(lu32* f, unsigned n) { while (*(volatile lu32*)f < n) __builtin_amdgcn_s_sleep(1); CBAR(); }
__device__ __forceinline__ void lds_wait_ge_slow(lu32* f, unsigned n) { while (*(volatile lu32*)f < n) __builtin_amdgcn_s_sleep(4); CBAR(); }
typedef __attribute__((address_space(3))) u32x2 lu32x2;
__device__ __forceinline__ unsigned prog_get(lu32* flags) { const u32x2 v = *(volatile lu32x2*)(flags + FL_PROG); return v[0] < v[1] ? v[0] : v[1]; }
__device__ __forceinline__ void prog_wait(lu32* flags, unsigned n) { while (prog_get(flags) < n) __builtin_amdgcn_s_sleep(1); CBAR(); }
__device__ __forceinline__ void prog_wait_slow(lu32* flags, unsigned n) { while (prog_get(flags) < n) __builtin_amdgcn_s_sleep(4); CBAR(); }
__device__ __forceinline__ void lds_inc(lu32* f, int lane) { if (lane == 0) __hip_atomic_fetch_add(f, 1u, __ATOMIC_RELAXED, __HIP_MEMORY_SCOPE_WORKGROUP); }
__device__ __forceinline__ void lds_signal(lu32* f, int lane) { LDSWAIT(); lds_inc(f, lane); }
__device__ __forceinline__ unsigned lds_ticket(lu32* f, int lane) {
    LDSWAIT(); unsigned old = 0;
    if (lane == 0) old = __hip_atomic_fetch_add(f, 1u, __ATOMIC_RELAXED, __HIP_MEMORY_SCOPE_WORKGROUP);
    old = __builtin_amdgcn_readfirstlane(old); CBAR(); return old;
}
__device__ __forceinline__ void ln16(float (&a)[8], float (&b)[8], bool has_b) {
    float s = 0.f;
#pragma unroll
    for (int k = 0; k < 8; ++k) s += a[k] + (has_b ? b[k] : 0.f);
    const float mean = wave_sum(s) * (1.f / D);
    float q = 0.f;
#pragma unroll
    for (int k = 0; k < 8; ++k) { const float d0 = a[k] - mean, d1 = has_b ? b[k] - mean : 0.f; q += d0 * d0 + d1 * d1; }
    const float var = rbf(wave_sum(q) * (1.f / D));
    const float mb = rbf(mean), den = rbf(__builtin_amdgcn_sqrtf(rbf(var + 1e-5f)));
    const float inv = 1.f / den;
#pragma unroll
    for (int k = 0; k < 8; ++k) { a[k] = rbf(rbf(a[k] - mb) * inv); b[k] = rbf(rbf(b[k] - mb) * inv); }
}

__device__ __forceinline__ int out_col(int j, int k) { return 64 * (j >> 4) + 8 * (2 * (j & 3) + (k >> 1)) + 2 * ((j >> 2) & 3) + (k & 1); }
__device__ __forceinline__ void final_out(unsigned char* lds, lu32* flags, int lane, int j, u16* out, rsrc_t rws, Tag tag) {
    if (lds_ticket(flags + FL_R, lane) == 7) {
        const int k = lane & 3;
        const float* red = (const float*)(lds + L_RED);
        const f32x4 x0 = *(const f32x4*)(red + k * 8), x1 = *(const f32x4*)(red + k * 8 + 4);
        const f32x2 xb = *(const f32x2*)(red + 80 + k * 2);
        const float A = ((x0[0] + x0[1]) + (x0[2] + x0[3])) + ((x1[0] + x1[1]) + x1[2]);
        const float res = rbf(rbf(xb[0] + rbf(A)) + rbf(rbf(x1[3]) + xb[1]));
        if (lane < 4) __builtin_amdgcn_raw_buffer_store_b16((u16)(__float_as_uint(res) >> 16), mk(out, D * 2), out_col(j, k) * 2, 0, 0);
        if (j == 0 && lane == 0) __builtin_amdgcn_raw_buffer_store_b32(tag.t32, rws, WS_EPOCH, 0, 16);
    }
}

typedef unsigned u32x16 __attribute__((ext_vector_type(16)));
template <typename T> __device__ __forceinline__ T* mkptr(unsigned lo, unsigned hi) { return (T*)(((unsigned long long)hi << 32) | lo); }
__global__ void __launch_bounds__(NTHR) fused_kernel(const u16* x_, const u16* kc_, const u16* vc_, const u16* wq_,
        const u16* wo_, const u16* w1_, const u16* b1_, const u16* w2_, const u16* b2_, u16* out_, unsigned char* ws_) {
    __shared__ __attribute__((aligned(16))) unsigned char lds[LDS_BYTES];
    u32x16 ka; u32x4 kb; u32x2 kd; unsigned long long pc0;
    asm volatile("s_load_dwordx16 %0, %4, 0x0\n\ts_load_dwordx4 %1, %4, 0x40\n\ts_load_dwordx2 %2, %4, 0x50\n\ts_getpc_b64 %3"
                 : "=&s"(ka), "=&s"(kb), "=&s"(kd), "=&s"(pc0) : "s"(__builtin_amdgcn_kernarg_segment_ptr()));
    const int tid = threadIdx.x, lane = tid & 63, wave = __builtin_amdgcn_readfirstlane(tid >> 6);
#define KARGS() asm volatile("s_waitcnt lgkmcnt(0)" : "+s"(ka), "+s"(kb), "+s"(kd)); \
    const u16* x = mkptr<const u16>(ka[0], ka[1]); const u16* kc = mkptr<const u16>(ka[2], ka[3]); const u16* vc = mkptr<const u16>(ka[4], ka[5]); const u16* wq = mkptr<const u16>(ka[6], ka[7]); \
    const u16* wo = mkptr<const u16>(ka[8], ka[9]); const u16* w1 = mkptr<const u16>(ka[10], ka[11]); const u16* b1 = mkptr<const u16>(ka[12], ka[13]); const u16* w2 = mkptr<const u16>(ka[14], ka[15]); \
    const u16* b2 = mkptr<const u16>(kb[0], kb[1]); u16* out = mkptr<u16>(kb[2], kb[3]); unsigned char* ws = mkptr<unsigned char>(kd[0], kd[1]); \
    (void)x; (void)kc; (void)vc; (void)wq; (void)wo; (void)w1; (void)b1; (void)w2; (void)b2; (void)out; (void)ws;
    const int b = blockIdx.x, g = 2 * (b & 7) + (b >> 7), s = (b >> 3) & 15;
    const bool isX = s < 7;
    const int u = 9 * g + (s - 7);
    const int r0 = s <= 5 ? 36 * s : 218, nrows = s < 5 ? 36 : 38;
    const int slot = isX ? g * 7 + s : 112 + u;
    const int nP = isX ? (s < 4 ? 76 : 72) : 56;
    const int nT = (nP + 7) >> 3;
    const int kbase = isX ? (s < 4 ? 304 * s : 1216 + 288 * (s - 4)) : 2080 + 224 * (s - 7);
    const size_t keyg = (size_t)g * SK + (SK - W) + kbase;
    const int nE = isX ? (s < 5 ? 42 : 44) : 36, nK0 = isX ? 18 + nE : 90, nV0 = nK0 + nP, nEnd = nV0 + nP;
    lu32* flags = (lu32*)(lds + L_FLAG);
    lu8* L = (lu8*)lds;
    const int r = lane >> 4, c = lane & 15, rg = lane >> 3, c8 = lane & 7;
    const unsigned h2base = WS_H2 + g * 16 * H2P;

    KARGS();
    if (__builtin_expect(wave < 2, 1)) {
        const int ld = wave;
        __builtin_amdgcn_s_setprio(3);
        volatile lu32* prog = flags + FL_PROG + ld;
        const unsigned tl0 = now32();
        bool live = false;
#define STEP(n_) do { VMWAIT(WL); if (!live && (n_) >= BARN) { LDSWAIT(); __builtin_amdgcn_s_barrier(); CBAR(); live = true; } if (live) *prog = (n_) + 4 - 2 * WL; \
            while (((now32() - tl0) << 3) < (unsigned)(((n_) + 4 - LEAD) * RATE)) {} } while (0)
        if (ld == 1 && (b >> 3) < CODEKB) __builtin_amdgcn_global_load_lds((const unsigned*)((const char*)(pc0 & ~63ull) + (b >> 3) * 1024 + lane * 16), (lu32*)(L + L_K + 70 * 1024), 16, 0, 0);
        if (ld == 0) {
            __builtin_amdgcn_global_load_lds((const unsigned*)((const char*)x + lane * 16), (lu32*)(L + L_XS), 16, 0, 0);
            __builtin_amdgcn_global_load_lds((const unsigned*)((const char*)x + 1024 + (lane & 31) * 4), (lu32*)(L + L_XS + 1024), 4, 0, 0);
            __builtin_amdgcn_global_load_lds((const unsigned*)(ws + WS_EPOCH), (lu32*)(L + L_EP), 4, 0, 16);
            if (lane < 32) flags[lane] = 0u;
        }
        {
            const char* p = (const char*)(wq + (size_t)((s >> 2) * 144 + 2 * rg + 16 * ld) * 4096 + g * 256 + (s & 3) * 64) + c8 * 16;
#pragma unroll
            for (int i = 0; i < 5; ++i) if (i < 4 || ld == 0) { GLDS(p + (size_t)i * 32 * 8192, L + L_W + (4 * i + 2 * ld) * 1024); GLDS(p + (size_t)i * 32 * 8192 + 8192, L + L_W + (4 * i + 2 * ld + 1) * 1024); }
        }
        if (!isX) {
            const int i0 = 2 * (1 ^ ld);
            const char* p = (const char*)(w1 + (size_t)(288 * (u & 1) + 2 * rg + 8 * i0) * F + 64 * (u >> 1)) + c8 * 16;
#pragma unroll 1
            for (int i = i0; i < 36; i += 4) { GLDS(p, L + L_E + i * 1024); GLDS(p + F * 2, L + L_E + (i + 1) * 1024); p += 32 * F * 2; STEP(18 + i); }
        }
        {
            const int n0 = isX ? 18 : 54, i0 = 2 * (1 ^ ld), dst = isX ? L_E : L_E2, last = (isX ? nrows : 32) * 1152 - 16;
            const char* p = isX ? (const char*)(wo + (size_t)(g * 256 + r0) * D) : (const char*)(w2 + (size_t)(32 * u) * D);
#pragma unroll 1
            for (int i = i0; i < nE; i += 4) {
                const int o0 = i * 1024 + lane * 16; const bool pad = isX && i == nE - 2;
                GLDS(p + (o0 < last ? o0 : last), L + dst + i * 1024); GLDS(p + (pad ? (o0 < last ? o0 : last) : o0 + 1024), L + dst + (pad ? i : i + 1) * 1024); STEP(n0 + i); }
        }
        const int cx = c ^ (r << 2);
        {   const int i0 = 2 * (((nK0 >> 1) ^ ld) & 1);
            const char* p = (const char*)(kc + keyg * DH) + r * 256; const unsigned sa = 16 * (cx ^ i0), sb = 1024 + 16 * (cx ^ (i0 + 1));
#pragma unroll 1
            for (int i = i0; i < nP; i += 4) { GLDS(p + sa + i * 1024, L + L_K + i * 1024); GLDS(p + sb + i * 1024, L + L_K + (i + 1) * 1024); STEP(nK0 + i); } }
        int nl;
        {   const int i0 = 2 * (((nV0 >> 1) ^ ld) & 1);
            const char* p = (const char*)(vc + keyg * DH) + r * 256; const unsigned sa = 16 * (cx ^ i0), sb = 1024 + 16 * (cx ^ (i0 + 1));
            lds_wait_ge(flags + FL_FE, NW); lds_wait_ge(flags + FL_FQ, 1);
            int i = i0;
#pragma unroll 1
            for (; i < nP; i += 4) { const int dst = i < 16 ? L_V1 + i * 1024 : L_W + (i - 16) * 1024;
                GLDS(p + sa + i * 1024, L + dst); GLDS(p + sb + i * 1024, L + dst + 1024); STEP(nV0 + i); }
            nl = nV0 + i;
        }
#define DR(k) if ((k) < WL) { VMWAIT(k); *prog = nl - 2 * (k); }
        DR(26) DR(24) DR(22) DR(20) DR(18) DR(16) DR(14) DR(12) DR(10) DR(8) DR(6) DR(4) DR(2)
        VMWAIT(0); *prog = 0x7fffffffu;
#undef DR
    } else if (wave == 2) {
        __builtin_amdgcn_s_setprio(2);
        const rsrc_t rws = mk(ws, WS_END);
        const int rb = s >> 2;
        VMWAIT(0);
        __builtin_amdgcn_s_barrier();
        CBAR();
        prog_wait(flags, 18);
        const u32x4 xa = *(const u32x4*)(lds + L_XS + lane * 16), xb = *(const u32x4*)(lds + L_XS + 1024 + c8 * 16);
        const unsigned epoch = *(const unsigned*)(lds + L_EP);
        float a[8], bb[8];
#pragma unroll
        for (int k = 0; k < 4; ++k) { a[2 * k] = bflo(xa[k]); a[2 * k + 1] = bfhi(xa[k]); bb[2 * k] = bflo(xb[k]); bb[2 * k + 1] = bfhi(xb[k]); }
        const bool has_b = lane < 8;
        ln16(a, bb, has_b);
        unsigned* xp = (unsigned*)(lds + L_XP); unsigned* yp = (unsigned*)(lds + L_YP);
        {
            u32x4 pa, pb;
#pragma unroll
            for (int k = 0; k < 4; ++k) { pa[k] = (__float_as_uint(a[2 * k]) >> 16) | __float_as_uint(a[2 * k + 1]); pb[k] = (__float_as_uint(bb[2 * k]) >> 16) | __float_as_uint(bb[2 * k + 1]); }
            *(u32x4*)(xp + 4 * lane) = pa;
            if (has_b) *(u32x4*)(xp + 256 + 4 * lane) = pb;
        }
        const Tag tag = mktag(epoch + 1u != 0u ? epoch + 1u : 1u);
        if (lane == 0) flags[FL_TAG] = tag.t32;
        LDSWAIT();
        unsigned long long tpub;
        {
            float acc[8];
#pragma unroll
            for (int k = 0; k < 8; ++k) acc[k] = 0.f;
#pragma unroll
            for (int i = 0; i < 9; ++i) {
                u32x4 wa = *(const u32x4*)(lds + L_W + (2 * i) * 1024 + lane * 16), wb = *(const u32x4*)(lds + L_W + (2 * i + 1) * 1024 + lane * 16);
                pair_perm(wa, wb); pair_fma(acc, wa, wb, xp[rb * 72 + rg + 8 * i]); }
            lds_signal(flags + FL_FQ, lane);
            float t4[4], t2[2];
#pragma unroll
            for (int k = 0; k < 4; ++k) t4[k] = rs32(acc[k], acc[k + 4]);
#pragma unroll
            for (int k = 0; k < 2; ++k) { t2[k] = rs16(t4[k], t4[k + 2]); t2[k] += dpp<ROR8>(t2[k]); }
            if ((lane & 8) == 0) st2(rws, WS_H1 + g * 8192 + (s * 32 + 8 * r + c8) * 16, t2[0], t2[1], tag);
            tpub = __builtin_amdgcn_s_memrealtime();
        }
        if (!isX) {
            ln16(a, bb, has_b);
            u32x4 pa, pb;
#pragma unroll
            for (int k = 0; k < 4; ++k) { pa[k] = (__float_as_uint(a[2 * k]) >> 16) | __float_as_uint(a[2 * k + 1]); pb[k] = (__float_as_uint(bb[2 * k]) >> 16) | __float_as_uint(bb[2 * k + 1]); }
            *(u32x4*)(yp + 4 * lane) = pa;
            if (has_b) *(u32x4*)(yp + 256 + 4 * lane) = pb;
        } else if (lane < 32) *(unsigned*)(lds + L_VEC + lane * 4) = 0u;
        if (lane < 32) { *(float*)(lds + L_AM + lane * 4) = -INFINITY; *(float*)(lds + L_AL + lane * 4) = 0.f; }
        lds_signal(flags + FL_Y, lane);
        {
            while (__builtin_amdgcn_s_memrealtime() - tpub < D1) __builtin_amdgcn_s_sleep(1);
            prog_wait(flags, QPROG);
            float va[8], vb[8]; unsigned qo[8];
#pragma unroll
            for (int i = 0; i < 8; ++i) qo[i] = WS_H1 + g * 8192 + (64 * i + lane) * 16;
            gather2p<8>(rws, qo, va, vb, tag, flags + FL_G, lane);
            unsigned* ql = (unsigned*)(lds + L_Q);
            const int gi = lane & 31, pr = 4 * (gi & 7) + (gi >> 3);
#pragma unroll
            for (int cbh = 0; cbh < 2; ++cbh)
                ql[64 * cbh + (lane & 32) + pr] = pkbf((va[cbh] + va[2 + cbh]) + (va[4 + cbh] + va[6 + cbh]), (vb[cbh] + vb[2 + cbh]) + (vb[4 + cbh] + vb[6 + cbh]));
        }
        lds_signal(flags + FL_Q, lane);
        lds_wait_ge(flags + FL_L, nT);
        {
            const float mw = *(const float*)(lds + L_AM + c * 8 + (r & 1) * 4);
            float M = fmaxf(mw, dpp<XOR1>(mw)); M = fmaxf(M, dpp<XOR2>(M)); M = fmaxf(M, dpp<HMIR>(M)); M = fmaxf(M, dpp<ROR8>(M));
            const float Ls = row_sum(*(const float*)(lds + L_AL + c * 8 + (r & 1) * 4) * __builtin_amdgcn_exp2f((mw - M) * LOG2E));
            if ((lane & 47) == 0) st2(rws, WS_FM + g * 512 + s * 32 + r * 16, M, Ls, tag);
        }
        if (isX) {
            const unsigned tref = now32();
            lds_wait_ge(flags + FL_E2, 1);
            bool okm = false, okt = lane >= 16; float mv = 0.f; int dmax = -0x40000000;
            for (unsigned spin = 0; spin < SPIN_CAP; ++spin) {
                u32x2 av = {0u, 0u}, t = {0u, 0u};
                if (!okm) atl(av, ws + WS_FM + g * 512 + lane * 8);
                if (!okt) atl(t, ws + WS_F2 + g * 128 + lane * 8);
                asm volatile("s_waitcnt vmcnt(0)" ::: "memory"); asm volatile("" : "+v"(av), "+v"(t));
                if (!okm && good(av[0], av[1], tag)) { okm = true; mv = dec(av[0]); }
                if (!okt && t[1] == tag.t32) { okt = true; dmax = (int)(t[0] - tref); }
                if (__all(okm & okt)) break;
            }
            float d = (float)dmax; d = fmaxf(d, dpp<XOR1>(d)); d = fmaxf(d, dpp<XOR2>(d)); d = fmaxf(d, dpp<HMIR>(d)); d = fmaxf(d, dpp<ROR8>(d)); d = fminf(d, 2000.f);
            const unsigned t2 = tref + (unsigned)(int)__builtin_bit_cast(float, __builtin_amdgcn_readfirstlane(__builtin_bit_cast(int, d))) + M2;
            if (lane == 0) *(volatile lu32*)(flags + FL_T2) = t2;
            lds_signal(flags + FL_T2V, lane);
            float M = fmaxf(mv, dpp<ROR4>(mv)); M = fmaxf(M, dpp<ROR8>(M)); M = x16_max(M);
            const float f = __builtin_amdgcn_exp2f((mv - M) * LOG2E);
            float den = f * dpp<XOR1>(mv); den += dpp<ROR4>(den); den += dpp<ROR8>(den); den = x16_sum(den);
            if ((lane & 1) == 0) *(float*)(lds + L_FT + ((lane >> 1) & 1) * 64 + (lane >> 2) * 4) = f * __builtin_amdgcn_rcpf(den);
            lds_signal(flags + FL_F, lane);
        } else {
            lds_wait_ge_slow(flags + FL_SN, 1);
            unsigned zo[5]; float za[5], zb[5];
#pragma unroll
            for (int i = 0; i < 5; ++i) zo[i] = WS_H3 + u * 8192 + 112 * 32 + i * 1024 + ((i < 4 || lane < 32) ? lane : 0) * 16;
            gather2<5>(rws, ws, zo, za, zb, tag);
            if (lane >= 32) { za[4] = 0.f; zb[4] = 0.f; }
            float t = rs32(((za[0] + za[1]) + (za[2] + za[3])) + za[4], ((zb[0] + zb[1]) + (zb[2] + zb[3])) + zb[4]);
            t = rs16(t, t); t += dpp<XOR2>(t); t += dpp<ROR4>(t); t += dpp<ROR8>(t);
            if ((lane & 30) == 0) ((float*)(lds + L_RED))[(2 * (lane & 1) + (lane >> 5)) * 8 + 7] = t;
            final_out(lds, flags, lane, u, out, rws, tag);
        }
    } else {
        const int w = wave - 3;
        const rsrc_t rws = mk(ws, WS_END);
        VMWAIT(0);
        __builtin_amdgcn_s_barrier();
        CBAR();
        const bool isR = !isX && w < 7;
        if (isR && w == 0) {
            const int ocol = out_col(u, lane & 3);
            const float xo = bflo(__builtin_amdgcn_raw_buffer_load_b16(mk(x, D * 2), ocol * 2, 0, 2)), b2o = bflo(__builtin_amdgcn_raw_buffer_load_b16(mk(b2, D * 2), ocol * 2, 0, 2));
            if (lane < 4) *(f32x2*)(lds + L_RED + (80 + 2 * lane) * 4) = f32x2{xo, b2o};
        }
        u32x4 e[6];
        lds_wait_ge_slow(flags + FL_Y, 1);
        const Tag tag = mktag(flags[FL_TAG]);
        if (!isX) {
            prog_wait_slow(flags, 18 + 4 * w + 4);
#pragma unroll
            for (int i = 0; i < 4; ++i) e[i] = *(const u32x4*)(lds + L_E + (4 * w + i) * 1024 + lane * 16);
            const unsigned* yp = (const unsigned*)(lds + L_YP) + 144 * (u & 1) + 16 * w;
            const unsigned y0 = yp[rg], y1 = yp[rg + 8];
            float h8[8];
#pragma unroll
            for (int k = 0; k < 8; ++k) h8[k] = 0.f;
            pair_perm(e[0], e[1]); pair_fma(h8, e[0], e[1], y0);
            pair_perm(e[2], e[3]); pair_fma(h8, e[2], e[3], y1);
            float t4[4], t2[2];
#pragma unroll
            for (int k = 0; k < 4; ++k) t4[k] = rs32(h8[k], h8[k + 4]);
#pragma unroll
            for (int k = 0; k < 2; ++k) { t2[k] = rs16(t4[k], t4[k + 2]); t2[k] += dpp<ROR8>(t2[k]); }
            float* hp = (float*)(lds + L_HP);
            if ((lane & 8) == 0) *(f32x2*)(hp + w * 64 + 8 * c8 + 2 * r) = f32x2{t2[0], t2[1]};
            lds_signal(flags + FL_H, lane);
        }
        f32x2 pre = {0.f, 0.f}; unsigned b1p = 0; unsigned long long th = 0;
        const bool hfin = !isX && w == NW - 1, mine = ((lane >> 4) & 1) == (u & 1);
        if (hfin) {
            b1p = __builtin_amdgcn_raw_buffer_load_b32(mk(b1, F * 2), (32 * u + 2 * (lane & 15)) * 2, 0, 2);
            lds_wait_ge(flags + FL_H, NW);
            const float* hp = (const float*)(lds + L_HP);
#pragma unroll
            for (int ww = 0; ww < NW; ++ww) pre += *(const f32x2*)(hp + ww * 64 + 2 * (lane & 31));
            if (lane < 32 && !mine) st2(rws, WS_HH + (u ^ 1) * 256 + (lane & 15) * 16, pre[0], pre[1], tag);
            th = __builtin_amdgcn_s_memrealtime();
        }
        prog_wait_slow(flags, nK0);
#pragma unroll
        for (int i = 0; i < 6; ++i) { const int row = 2 * (rg + 8 * (i >> 1)) + (i & 1);
            e[i] = *(const u32x4*)(lds + (isX ? L_E : L_E2) + (row < (isX ? nrows : 32) ? row : 0) * 1152 + w * 128 + c8 * 16); }
        lds_signal(flags + FL_FE, lane);
        pair_perm(e[0], e[1]); pair_perm(e[2], e[3]); pair_perm(e[4], e[5]);
        PIN4(e[0]); PIN4(e[1]); PIN4(e[2]); PIN4(e[3]); PIN4(e[4]); PIN4(e[5]);
        if (w < nT) {
            lds_wait_ge(flags + FL_Q, 1);
            u32x4 qf[4];
#pragma unroll
            for (int kk = 0; kk < 4; ++kk) qf[kk] = *(const u32x4*)(lds + L_Q + (c & 1) * 256 + kk * 64 + r * 16);
            const bool z0 = (c & 14) != 0, z1 = (c & 14) != 8;
            u32x4 qa0[4], qa1[4];
#pragma unroll
            for (int kk = 0; kk < 4; ++kk) { qa0[kk] = u32x4{z0 ? 0u : qf[kk][0], z0 ? 0u : qf[kk][1], z0 ? 0u : qf[kk][2], z0 ? 0u : qf[kk][3]};
                                             qa1[kk] = u32x4{z1 ? 0u : qf[kk][0], z1 ? 0u : qf[kk][1], z1 ? 0u : qf[kk][2], z1 ? 0u : qf[kk][3]}; }
            const int ksw = ((c & 3) << 2) | ((c >> 2) & 3);
#pragma unroll 1
            for (int tl = w; tl < nT; tl += NW) {
            prog_wait(flags, nK0 + (tl < 9 ? 8 * tl + 8 : 76));
            f32x4 sc = {0.f, 0.f, 0.f, 0.f}, sd = {0.f, 0.f, 0.f, 0.f};
#pragma unroll
            for (int kk = 0; kk < 4; ++kk) {
                const u32x4 k0 = *(const u32x4*)(lds + L_K + (32 * tl + c) * 256 + 16 * ((4 * kk + r) ^ ksw)), k1 = *(const u32x4*)(lds + L_K + (32 * tl + (tl < 9 ? 16 : 0) + c) * 256 + 16 * ((4 * kk + r) ^ ksw));
                sc = __builtin_amdgcn_mfma_f32_16x16x32_bf16(__builtin_bit_cast(bf16x8, qa0[kk]), __builtin_bit_cast(bf16x8, k0), sc, 0, 0, 0);
                sd = __builtin_amdgcn_mfma_f32_16x16x32_bf16(__builtin_bit_cast(bf16x8, qa1[kk]), __builtin_bit_cast(bf16x8, k1), sd, 0, 0, 0);
            }
            float sraw;
            { const float h0 = sc[0] + sd[0], h1 = sc[1] + sd[1];
              auto t = __builtin_amdgcn_permlane16_swap(__float_as_uint(h0), __float_as_uint(h1), false, false); sraw = __uint_as_float(t[0]); }
            const float sv = (tl == 9 && r >= 2) ? -INFINITY : rbf(rbf(sraw) * 0.08838834764831845f);
            float mh = fmaxf(sv, dpp<XOR1>(sv)); mh = fmaxf(mh, dpp<XOR2>(mh)); mh = fmaxf(mh, dpp<HMIR>(mh)); mh = fmaxf(mh, dpp<ROR8>(mh));
            { auto t = __builtin_amdgcn_permlane32_swap(__float_as_uint(mh), __float_as_uint(mh), false, false); mh = fmaxf(__uint_as_float(t[0]), __uint_as_float(t[1])); }
            const float p = __builtin_amdgcn_exp2f((sv - mh) * LOG2E);
            *(u16*)(lds + L_P + (r & 1) * 640 + (32 * tl + 16 * (r >> 1) + c) * 2) = (u16)(pkbf(p, p) & 0xffffu);
            { float l = row_sum(p); l = rs32(l, l);
              if ((lane & 47) == 0) { *(float*)(lds + L_AM + tl * 8 + r * 4) = mh; *(float*)(lds + L_AL + tl * 8 + r * 4) = l; } }
            lds_signal(flags + FL_L, lane);
            }
        } else if (hfin) {
            while (__builtin_amdgcn_s_memrealtime() - th < 100) __builtin_amdgcn_s_sleep(1);
            prog_wait_slow(flags, HPROG);
            float pa[1], pb[1]; const unsigned po[1] = {WS_HH + u * 256 + (lane & 15) * 16};
            gather2p<1>(rws, po, pa, pb, tag, flags + FL_G, lane);
            float hv[2];
#pragma unroll
            for (int k = 0; k < 2; ++k) {
                const float mine_r = dec(enc(pre[k], tag));
                const float t = rbf(rbf(mine_r + (k ? pb[0] : pa[0])) + (k ? bfhi(b1p) : bflo(b1p)));
                const float inner = 0.7978845608028654f * (t + 0.044715f * t * t * t);
                const float th = 1.f - 2.f / (1.f + __builtin_amdgcn_exp2f(inner * (2.f * LOG2E)));
                hv[k] = 0.5f * t * (1.f + th);
            }
            if (lane < 32 && mine) *(unsigned*)(lds + L_VEC + (lane & 15) * 4) = pkbf(hv[0], hv[1]);
            lds_signal(flags + FL_V, lane);
        }
#define OUT_DOT() do { \
            float acc[8]; \
            _Pragma("unroll") for (int k = 0; k < 8; ++k) acc[k] = 0.f; \
            const unsigned* vec = (const unsigned*)(lds + L_VEC); \
            const unsigned v0 = vec[rg], v1 = vec[rg + 8], v2 = isX ? vec[rg + 16] : 0u; \
            pair_fma(acc, e[0], e[1], v0); pair_fma(acc, e[2], e[3], v1); pair_fma(acc, e[4], e[5], v2); \
            float t4[4], t2[2]; \
            _Pragma("unroll") for (int k = 0; k < 4; ++k) t4[k] = rs32(acc[k], acc[k + 4]); \
            _Pragma("unroll") for (int k = 0; k < 2; ++k) { t2[k] = rs16(t4[k], t4[k + 2]); t2[k] += dpp<ROR8>(t2[k]); } \
              \
            if ((lane & 8) == 0) st2(rws, WS_H3 + ((4 * w + r) * 4 + (c8 >> 1)) * 8192 + slot * 32 + (c8 & 1) * 16, t2[0], t2[1], tag); \
        } while (0)
        if (w < NA) {
            lds_wait_ge(flags + FL_L, nT);
            u32x4 pf[10];
            const bool prow = (c & 11) == 0;
#pragma unroll
            for (int ks = 0; ks < 10; ++ks) { const u32x4 t = *(const u32x4*)(lds + L_P + ((c >> 2) & 1) * 640 + (32 * ks + 8 * r) * 2);
                pf[ks] = u32x4{prow ? t[0] : 0u, prow ? t[1] : 0u, prow ? t[2] : 0u, prow ? t[3] : 0u}; }
            const unsigned lbase = (unsigned)(size_t)L;
            const int tq = (lane & 15) >> 2, tch = 2 * w + ((lane & 3) >> 1);
            const unsigned tra0 = lbase + (8 * r + tq) * 256 + 16 * (tch ^ ((tq << 2) | ((2 * r) & 3))) + 8 * (lane & 1);
            const unsigned tra1 = lbase + (8 * r + 4 + tq) * 256 + 16 * (tch ^ ((tq << 2) | ((2 * r + 1) & 3))) + 8 * (lane & 1);
            float o = 0.f, scl[10];
            {   float mt[10];
#pragma unroll
                for (int ks = 0; ks < 10; ++ks) mt[ks] = *(const float*)(lds + L_AM + ks * 8 + (r & 1) * 4);
                const float M = fmaxf(fmaxf(fmaxf(fmaxf(mt[0], mt[1]), fmaxf(mt[2], mt[3])), fmaxf(fmaxf(mt[4], mt[5]), fmaxf(mt[6], mt[7]))), fmaxf(mt[8], mt[9]));
#pragma unroll
                for (int ks = 0; ks < 10; ++ks) scl[ks] = __builtin_amdgcn_exp2f((mt[ks] - M) * LOG2E);
            }
#define TRD(T, AD) "ds_read_b64_tr_b16 " T ", " AD "\n\t"
            unsigned have = 0;
#define NEED(n_, SPIN) if (have < (unsigned)(n_)) { do { have = __builtin_amdgcn_readfirstlane(prog_get(flags)); if (have >= (unsigned)(n_)) break; if (!(SPIN)) __builtin_amdgcn_s_sleep(1); } while (true); CBAR(); }
#define HINT(ka) if ((ka) + HT == nT - 1 && w == 0 && lane == 0) { __builtin_amdgcn_raw_buffer_store_b64(u32x2{now32() + P2A + (((nEnd - (have < (unsigned)nEnd ? have : nEnd)) * P2B) >> 3), tag.t32}, rws, WS_F2 + g * 128 + s * 8, 0, 16); *(volatile lu32*)(flags + FL_E2) = 1u; }
#define PVM(ka, A, B) o = fmaf(scl[ka], __builtin_amdgcn_mfma_f32_16x16x32_bf16(__builtin_bit_cast(bf16x8, pf[ka]), __builtin_bit_cast(bf16x8, u32x4{A[0], A[1], B[0], B[1]}), f32x4{0.f, 0.f, 0.f, 0.f}, 0, 0, 0)[0], o)
#define PV1(ka) { u32x2 t0, t1; NEED(nV0 + ((ka) < 9 ? 8 * (ka) + 8 : 76), (ka) + 1 == nT) HINT(ka) \
                asm volatile(TRD("%0", "%2") TRD("%1", "%3") "s_waitcnt lgkmcnt(0)" : "=&v"(t0), "=&v"(t1) : "v"(tra0 + vtile(ka) - (((ka) == 9 && r >= 2) ? 4096 : 0)), "v"(tra1 + vtile(ka) - (((ka) == 9 && r >= 2) ? 4096 : 0)) : "memory"); \
                PVM(ka, t0, t1); }
#define PV3(ka) { u32x2 t0, t1, t2, t3, t4, t5; NEED(nV0 + 8 * (ka) + 24, false) \
                asm volatile(TRD("%0", "%6") TRD("%1", "%7") TRD("%2", "%8") TRD("%3", "%9") TRD("%4", "%10") TRD("%5", "%11") "s_waitcnt lgkmcnt(0)" : "=&v"(t0), "=&v"(t1), "=&v"(t2), "=&v"(t3), "=&v"(t4), "=&v"(t5) \
                    : "v"(tra0 + vtile(ka)), "v"(tra1 + vtile(ka)), "v"(tra0 + vtile((ka) + 1)), "v"(tra1 + vtile((ka) + 1)), "v"(tra0 + vtile((ka) + 2)), "v"(tra1 + vtile((ka) + 2)) : "memory"); \
                PVM(ka, t0, t1); PVM((ka) + 1, t2, t3); PVM((ka) + 2, t4, t5); }
            PV3(0) PV1(3) PV1(4) PV1(5) PV1(6)
            if (nT > 7) { PV1(7) PV1(8) }
            if (nT > 9) PV1(9)
            if (lane < 32) st1(rws, h2base + s * H2P + (r * 128 + 16 * w + c) * 8, o, tag);
            if (isX && w < 3) {
                const bool live = 16 * w + 2 * c8 < nrows; const int row = r0 + 16 * w + (live ? 2 * c8 : 0);
                unsigned off[2]; float va[2], vb[2], f[2];
#pragma unroll
                for (int i = 0; i < 2; ++i) off[i] = h2base + (8 * i + rg) * H2P + row * 8;
                lds_spin_ge(flags + FL_T2V, 1);
                wait_until(*(volatile lu32*)(flags + FL_T2));
                gather2p<2>(rws, off, va, vb, tag, flags + FL_RI, lane);
                lds_spin_ge(flags + FL_F, 1);
#pragma unroll
                for (int i = 0; i < 2; ++i) f[i] = *(const float*)(lds + L_FT + ((row >> 7) & 1) * 64 + (8 * i + rg) * 4);
                float ov = rs32(f[0] * va[0] + f[1] * va[1], f[0] * vb[0] + f[1] * vb[1]);
                ov = rs16(ov, ov); ov += dpp<ROR8>(ov);
                if ((lane & 24) == 0 && live) *(u16*)(lds + L_VEC + (16 * w + 2 * c8 + (lane >> 5)) * 2) = (u16)(pkbf(ov, ov) & 0xffffu);
                lds_signal(flags + FL_V, lane);
            }
        }
        if (isX) { lds_wait_ge_slow(flags + FL_RI, 3); lds_spin_ge(flags + FL_V, 3); } else { prog_wait_slow(flags, nEnd); lds_wait_ge(flags + FL_V, 1); }
        OUT_DOT();
        if (isR) {
            unsigned zo[1] = {WS_H3 + u * 8192 + w * 512 + lane * 8}; float z[1] = {0.f}; bool ok[1] = {false};
            for (unsigned spin = 0; spin < SPIN_CAP; ++spin) {
                u32x2 sv = {0u, 0u};
                if (lane == 0) atl(sv, ws + zo[0]);
                asm volatile("s_waitcnt vmcnt(0)" ::: "memory"); asm volatile("" : "+v"(sv));
                if (__builtin_amdgcn_readfirstlane(sv[1]) == tag.t32) break;
                if (*(volatile lu32*)(flags + FL_SN) != 0u) break;
            }
            lds_inc(flags + FL_SN, lane);
            apoll<1>(ws, zo, z, ok, tag);
            float t = z[0]; t += dpp<ROR4>(t); t += dpp<ROR8>(t); t = x16_sum(t);
            if (lane < 4) ((float*)(lds + L_RED))[lane * 8 + w] = t;
            final_out(lds, flags, lane, u, out, rws, tag);
        }
    }
}

extern "C" void kernel_launch(void* const* d_in, const int* in_sizes, int n_in, void* d_out, int out_size, void* d_ws, size_t ws_size, hipStream_t stream) {
    fused_kernel<<<256, NTHR, 0, stream>>>((const u16*)d_in[0], (const u16*)d_in[1], (const u16*)d_in[2], (const u16*)d_in[3], (const u16*)d_in[4],
        (const u16*)d_in[6], (const u16*)d_in[7], (const u16*)d_in[8], (const u16*)d_in[9], (u16*)d_out, (unsigned char*)d_ws);
}
```
